```python
import torch

def _softmax(x, axis=-1):
    return torch.softmax(x, dim=axis)
def _gelu(x):
    return torch.nn.functional.gelu(x, approximate="tanh")
def _silu(x):
    return torch.nn.functional.silu(x)
def _sigmoid(x):
    return torch.sigmoid(x)
def _layernorm(x, axis=-1, eps=1e-5):
    m = x.mean(dim=axis, keepdim=True)
    v = x.var(dim=axis, keepdim=True, unbiased=False)
    return (x - m) / torch.sqrt(v + eps)
def _scatter_add(tgt, idx, src, axis=0):
    return tgt.clone().index_add_(axis, idx, src)
def _segment_sum(src, seg, n):
    out = torch.zeros((n,) + src.shape[1:], dtype=src.dtype)
    return out.index_add_(0, seg, src)
def _pad(x, pw):
    flat = tuple(v for pair in reversed(pw) for v in pair)
    return torch.nn.functional.pad(x, flat)

kernel_name = 'standalone__gqa_block_cached__decode'

def setup_inputs():

    _w = (lambda a: (lambda t: t.to(torch.bfloat16) if t.is_floating_point() else t)(torch.as_tensor(a)))
    B, Sq, D = 2, 1, 576
    Hq, Hkv, Sk, Dh = 32, 16, 32768, 128
    assert Hq % Hkv == 0, (Hq, Hkv)
    return {
        "x": _w((lambda *s: torch.randn(*s, dtype=torch.bfloat16) * 0.8)(B, Sq, D)),
        "k_cache": _w((lambda *s: torch.randn(*s, dtype=torch.bfloat16) * 0.8)(B, Hkv, Sk, Dh)),
        "v_cache": _w((lambda *s: torch.randn(*s, dtype=torch.bfloat16) * 0.05)(B, Hkv, Sk, Dh)),
        "wq": _w((lambda *s: torch.randn(*s, dtype=torch.bfloat16) * 0.8)(D, Hq * Dh) * (D ** -0.5)),
        "wo": _w((lambda *s: torch.randn(*s, dtype=torch.bfloat16) * 0.05)(Hq * Dh, D) * ((Hq * Dh) ** -0.5)),
    }

def reference(x, k_cache, v_cache, wq, wo):
    B, Sq, D = x.shape
    Hq, Dh = 32, 128
    Hkv = k_cache.shape[1]
    rep = Hq // Hkv
    q = (lambda x, *d: x.permute(*d))((x @ wq).reshape(B, Sq, Hq, Dh), 0, 2, 1, 3)
    k = (lambda x, n, axis: torch.repeat_interleave(x, n, dim=axis))(k_cache, rep, axis=1)
    v = (lambda x, n, axis: torch.repeat_interleave(x, n, dim=axis))(v_cache, rep, axis=1)
    scores = torch.einsum("bhqd,bhkd->bhqk", q, k) * (Dh ** -0.5)
    p = _softmax(scores, axis=-1)
    o = torch.einsum("bhqk,bhkd->bhqd", p, v)
    return (lambda x, *d: x.permute(*d))(o, 0, 2, 1, 3).reshape(B, Sq, Hq * Dh) @ wo
```

```cpp
#include <hip/hip_runtime.h>
#include <hip/hip_bf16.h>
#include <stdint.h>

using u32x4  = __attribute__((ext_vector_type(4))) unsigned;
using f32x4  = __attribute__((ext_vector_type(4))) float;
using bf16x2 = __attribute__((ext_vector_type(2))) __bf16;
using bf16x8 = __attribute__((ext_vector_type(8))) __bf16;
typedef __attribute__((address_space(1))) unsigned gu32;
typedef __attribute__((address_space(1))) unsigned long long gu64;

constexpr int NB = 2, DM = 576, HQ = 32, HKV = 16, SK = 32768, DH = 128;
constexpr int NSPLIT = 8;
constexpr int KEYS_WG = SK / NSPLIT;
constexpr int NWAVE = 8;
constexpr int PAGE = 32;
constexpr int NIT = KEYS_WG / (PAGE * NWAVE);
constexpr int QCOLS = HQ * DH;
constexpr float C2 = 0.08838834764831845f * 1.4426950408889634f;

constexpr size_t WS_CNT = 0;
constexpr size_t WS_PML = 4096;
constexpr size_t WS_PO  = WS_PML + (size_t)NB * HKV * 2 * NSPLIT * 8;
constexpr size_t WS_Y   = WS_PO + (size_t)NB * HKV * 2 * NSPLIT * DH * 4;

__device__ __forceinline__ float dot2(unsigned k, unsigned q, float acc) {
  return __builtin_amdgcn_fdot2_f32_bf16(__builtin_bit_cast(bf16x2, k), __builtin_bit_cast(bf16x2, q), acc, false);
}
__device__ __forceinline__ unsigned cvtpk(float lo, float hi) {
  unsigned r; asm("v_cvt_pk_bf16_f32 %0, %1, %2" : "=v"(r) : "v"(lo), "v"(hi)); return r;
}
template <int CTRL> __device__ __forceinline__ float dpp(float x) {
  return __builtin_bit_cast(float, __builtin_amdgcn_mov_dpp(__builtin_bit_cast(int, x), CTRL, 0xf, 0xf, true));
}
template <int CTRL> __device__ __forceinline__ unsigned dppu(unsigned x) {
  return (unsigned)__builtin_amdgcn_mov_dpp((int)x, CTRL, 0xf, 0xf, true);
}
constexpr int XOR1 = 0xB1, XOR2 = 0x4E, XOR7 = 0x141, XOR8 = 0x128, ROR4 = 0x124, BCAST = 0x150;
__device__ __forceinline__ float xrow16_max(float x) {
  auto s = __builtin_amdgcn_permlane16_swap(__float_as_uint(x), __float_as_uint(x), false, false);
  x = fmaxf(__uint_as_float(s[0]), __uint_as_float(s[1]));
  auto t = __builtin_amdgcn_permlane32_swap(__float_as_uint(x), __float_as_uint(x), false, false);
  return fmaxf(__uint_as_float(t[0]), __uint_as_float(t[1]));
}
__device__ __forceinline__ f32x4 sel_mfma(unsigned P2, const u32x4& sel, const u32x4& bfrag, f32x4 acc) {
  u32x4 a;
  a[0] = __builtin_amdgcn_perm(P2, P2, sel[0]);
  a[1] = __builtin_amdgcn_perm(P2, P2, sel[1]);
  a[2] = __builtin_amdgcn_perm(P2, P2, sel[2]);
  a[3] = __builtin_amdgcn_perm(P2, P2, sel[3]);
  return __builtin_amdgcn_mfma_f32_16x16x32_bf16(__builtin_bit_cast(bf16x8, a), __builtin_bit_cast(bf16x8, bfrag), acc, 0, 0, 0);
}
__device__ __forceinline__ u32x4 make_sel(int lane) {
  const int m = lane & 15, g = m >> 3, j = m & 7;
  const unsigned bytes = g ? 0x0302u : 0x0100u;
  const unsigned v = (j & 1) ? ((bytes << 16) | 0x0c0cu) : (0x0c0c0000u | bytes);
  u32x4 s;
  s[0] = ((j >> 1) == 0) ? v : 0x0c0c0c0cu;
  s[1] = ((j >> 1) == 1) ? v : 0x0c0c0c0cu;
  s[2] = ((j >> 1) == 2) ? v : 0x0c0c0c0cu;
  s[3] = ((j >> 1) == 3) ? v : 0x0c0c0c0cu;
  return s;
}
__device__ __forceinline__ void lds_barrier() {
  asm volatile("s_waitcnt lgkmcnt(0)" ::: "memory");
  __builtin_amdgcn_s_barrier();
  asm volatile("" ::: "memory");
}
__device__ __forceinline__ u32x4 ldnt(const u32x4* p) { return __builtin_nontemporal_load(p); }

constexpr int L_XT = 0;
constexpr int L_QP = 2304;
constexpr int L_MR = L_QP + 4096;
constexpr int MR_REC = 2 * DH + 4;
constexpr int L_END = L_MR + NWAVE * MR_REC * 4;
constexpr int LDS_MAIN = 84 * 1024;
static_assert(L_END <= LDS_MAIN, "lds");

__global__ void __launch_bounds__(512) k_main(const unsigned short* __restrict__ x, const unsigned short* __restrict__ kc,
                                              const unsigned short* __restrict__ vc, const unsigned short* __restrict__ wq,
                                              unsigned char* __restrict__ ws) {
  extern __shared__ __attribute__((aligned(16))) unsigned char smem[];
  const int tid = threadIdx.x, lane = tid & 63, wave = __builtin_amdgcn_readfirstlane(tid >> 6);
  const int r = lane >> 4, c = lane & 15;
  const int bid = blockIdx.x, xcd = bid & 7, idx = bid >> 3;
  const int kvh = 2 * xcd + (idx & 1), b = (idx >> 1) & 1, split = idx >> 2;

  unsigned xv = 0;
  if (tid < DM / 2) xv = reinterpret_cast<const unsigned*>(x)[b * (DM / 2) + tid];
  const int hf = wave & 1, rg = wave >> 1;
  const u32x4* wqp = reinterpret_cast<const u32x4*>(wq + (size_t)(rg * 144 + r) * QCOLS + kvh * 256 + hf * 128 + 8 * c);
  u32x4 wr[36];
#pragma unroll
  for (int i = 0; i < 36; ++i) wr[i] = wqp[(size_t)i * 4 * QCOLS / 8];
  __builtin_amdgcn_sched_barrier(0);
  const size_t kvbase = ((size_t)(b * HKV + kvh) * SK + (size_t)split * KEYS_WG + (size_t)wave * PAGE + r) * DH + 8 * c;
  const u32x4* kp = reinterpret_cast<const u32x4*>(kc + kvbase);
  const u32x4* vp = reinterpret_cast<const u32x4*>(vc + kvbase);
  constexpr int ISTR = 4 * DH / 8;
  constexpr int PSTR = NWAVE * PAGE * DH / 8;
  u32x4 kr[8], vr[8];
#pragma unroll
  for (int i = 0; i < 8; ++i) kr[i] = ldnt(kp + i * ISTR);
#pragma unroll
  for (int i = 0; i < 8; ++i) vr[i] = ldnt(vp + i * ISTR);
  __builtin_amdgcn_sched_barrier(0);

  if (bid == 0 && tid < 8) reinterpret_cast<unsigned*>(ws + WS_CNT)[tid * 32] = 0u;

  const u32x4 sel = make_sel(lane);
  unsigned* xt = reinterpret_cast<unsigned*>(smem + L_XT);
  if (tid < DM / 2) {
    const int k0 = 2 * tid, k1 = k0 + 1;
    xt[(k0 & 3) * 144 + (k0 >> 2)] = (xv & 0xffffu) | (xv << 16);
    xt[(k1 & 3) * 144 + (k1 >> 2)] = (xv >> 16) | (xv & 0xffff0000u);
  }
  lds_barrier();
  f32x4 qa = {0.f, 0.f, 0.f, 0.f};
  {
    const u32x4* xr = reinterpret_cast<const u32x4*>(xt + r * 144 + rg * 36);
#pragma unroll
    for (int u = 0; u < 9; ++u) {
      const u32x4 xx = xr[u];
#pragma unroll
      for (int e = 0; e < 4; ++e) qa = sel_mfma(xx[e], sel, wr[4 * u + e], qa);
    }
  }
  float* qp = reinterpret_cast<float*>(smem + L_QP);
  if (r < 2) *reinterpret_cast<f32x4*>(qp + wave * 128 + 8 * c + 4 * r) = qa;
  lds_barrier();
  u32x4 qpk[2];
#pragma unroll
  for (int g = 0; g < 2; ++g) {
    f32x4 lo = {0.f, 0.f, 0.f, 0.f}, hi = {0.f, 0.f, 0.f, 0.f};
#pragma unroll
    for (int w4 = 0; w4 < 4; ++w4) {
      lo += *reinterpret_cast<const f32x4*>(qp + (2 * w4 + g) * 128 + 8 * c);
      hi += *reinterpret_cast<const f32x4*>(qp + (2 * w4 + g) * 128 + 8 * c + 4);
    }
    qpk[g][0] = cvtpk(lo[0], lo[1]); qpk[g][1] = cvtpk(lo[2], lo[3]);
    qpk[g][2] = cvtpk(hi[0], hi[1]); qpk[g][3] = cvtpk(hi[2], hi[3]);
  }

  float m = -INFINITY, l = 0.f;
  f32x4 acc = {0.f, 0.f, 0.f, 0.f};
  for (int it = 0; it < NIT; ++it) {
    float xs[16];
#pragma unroll
    for (int i = 0; i < 8; ++i)
#pragma unroll
      for (int g = 0; g < 2; ++g) {
        float d = 0.f;
#pragma unroll
        for (int k = 0; k < 4; ++k) d = dot2(kr[i][k], qpk[g][k], d);
        xs[2 * i + g] = d;
      }
    __builtin_amdgcn_sched_barrier(0);
    kp += PSTR;
    if (it + 1 < NIT) {
#pragma unroll
      for (int i = 0; i < 8; ++i) kr[i] = ldnt(kp + i * ISTR);
    }
    __builtin_amdgcn_sched_barrier(0);
#define TR_STEP(HALF, CTRL, BIT) _Pragma("unroll") for (int h = 0; h < (HALF); ++h) { \
      const float lo_ = xs[h], hi_ = xs[h + (HALF)]; const float t1_ = lo_ + dpp<CTRL>(lo_), t2_ = hi_ + dpp<CTRL>(hi_); \
      xs[h] = (c & (BIT)) ? t2_ : t1_; }
    TR_STEP(8, XOR8, 8) TR_STEP(4, XOR7, 4) TR_STEP(2, XOR2, 2) TR_STEP(1, XOR1, 1)
#undef TR_STEP
    const float sc = xs[0] * C2;
    float pm = fmaxf(sc, dpp<XOR2>(sc));
    pm = fmaxf(pm, dpp<ROR4>(pm));
    pm = fmaxf(pm, dpp<XOR8>(pm));
    pm = xrow16_max(pm);
    const float mn = fmaxf(m, pm);
    const float alpha = __builtin_amdgcn_exp2f(m - mn);
    const float p = __builtin_amdgcn_exp2f(sc - mn);
    l = fmaf(l, alpha, p);
    m = mn;
    const float a0 = dpp<0x00>(alpha), a1 = dpp<0x55>(alpha);
    const float aacc = (lane < 32) ? a0 : a1;
    acc *= aacc;
    const unsigned P2 = cvtpk(p, dpp<XOR1>(p));
    acc = sel_mfma(dppu<BCAST + 0>(P2), sel, vr[0], acc);
    acc = sel_mfma(dppu<BCAST + 2>(P2), sel, vr[1], acc);
    acc = sel_mfma(dppu<BCAST + 4>(P2), sel, vr[2], acc);
    acc = sel_mfma(dppu<BCAST + 6>(P2), sel, vr[3], acc);
    acc = sel_mfma(dppu<BCAST + 8>(P2), sel, vr[4], acc);
    acc = sel_mfma(dppu<BCAST + 10>(P2), sel, vr[5], acc);
    acc = sel_mfma(dppu<BCAST + 12>(P2), sel, vr[6], acc);
    acc = sel_mfma(dppu<BCAST + 14>(P2), sel, vr[7], acc);
    __builtin_amdgcn_sched_barrier(0);
    vp += PSTR;
    if (it + 1 < NIT) {
#pragma unroll
      for (int i = 0; i < 8; ++i) vr[i] = ldnt(vp + i * ISTR);
    }
    __builtin_amdgcn_sched_barrier(0);
  }
  l += dpp<XOR2>(l); l += dpp<ROR4>(l); l += dpp<XOR8>(l);
  {
    auto s = __builtin_amdgcn_permlane16_swap(__float_as_uint(l), __float_as_uint(l), false, false);
    l = __uint_as_float(s[0]) + __uint_as_float(s[1]);
    auto t = __builtin_amdgcn_permlane32_swap(__float_as_uint(l), __float_as_uint(l), false, false);
    l = __uint_as_float(t[0]) + __uint_as_float(t[1]);
  }
  float* mr = reinterpret_cast<float*>(smem + L_MR);
  {
    float* rec = mr + wave * MR_REC;
    *reinterpret_cast<f32x4*>(rec + 4 + (r >> 1) * 128 + 8 * c + 4 * (r & 1)) = acc;
    if (lane < 2) { rec[2 * lane] = m; rec[2 * lane + 1] = l; }
  }
  lds_barrier();
  if (tid < 256) {
    const int g = tid >> 7, d = tid & 127;
    float M = -INFINITY;
#pragma unroll
    for (int w = 0; w < NWAVE; ++w) M = fmaxf(M, mr[w * MR_REC + 2 * g]);
    float L = 0.f, o = 0.f;
#pragma unroll
    for (int w = 0; w < NWAVE; ++w) {
      const float wt = __builtin_amdgcn_exp2f(mr[w * MR_REC + 2 * g] - M);
      L = fmaf(wt, mr[w * MR_REC + 2 * g + 1], L);
      o = fmaf(wt, mr[w * MR_REC + 4 + g * 128 + d], o);
    }
    const size_t slot = (size_t)((b * HKV + kvh) * 2 + g) * NSPLIT + split;
    reinterpret_cast<float*>(ws + WS_PO)[slot * DH + d] = o;
    if (d == 0) reinterpret_cast<float2*>(ws + WS_PML)[slot] = make_float2(M, L);
  }
}

__global__ void __launch_bounds__(256) k_tail(const unsigned short* __restrict__ wo, unsigned char* __restrict__ ws,
                                              unsigned short* __restrict__ out) {
  __shared__ __attribute__((aligned(16))) unsigned short o16[2][256];
  __shared__ __attribute__((aligned(16))) float yp[4][2][128];
  __shared__ unsigned lastflag;
  const int tid = threadIdx.x, lane = tid & 63, wave = __builtin_amdgcn_readfirstlane(tid >> 6);
  const int r = lane >> 4, c = lane & 15;
  const int ch = blockIdx.x & 7, kvh = blockIdx.x >> 3;
  const int cc = c < 9 ? c : 8;
  const u32x4* wp = reinterpret_cast<const u32x4*>(wo + (size_t)(kvh * 256 + wave * 64 + r) * DM + 72 * ch + 8 * cc);
  u32x4 wr[16];
#pragma unroll
  for (int i = 0; i < 16; ++i) wr[i] = wp[(size_t)i * 4 * DM / 8];
  {
    const int bb = tid >> 7, g = (tid >> 6) & 1, d0 = 2 * (tid & 63);
    const size_t slot0 = (size_t)((bb * HKV + kvh) * 2 + g) * NSPLIT;
    const float2* pml = reinterpret_cast<const float2*>(ws + WS_PML) + slot0;
    const float* po = reinterpret_cast<const float*>(ws + WS_PO) + slot0 * DH + d0;
    float2 ml[NSPLIT], a[NSPLIT];
#pragma unroll
    for (int s = 0; s < NSPLIT; ++s) { ml[s] = pml[s]; a[s] = *reinterpret_cast<const float2*>(po + s * DH); }
    float M = -INFINITY;
#pragma unroll
    for (int s = 0; s < NSPLIT; ++s) M = fmaxf(M, ml[s].x);
    float L = 0.f, o0 = 0.f, o1 = 0.f;
#pragma unroll
    for (int s = 0; s < NSPLIT; ++s) {
      const float w = __builtin_amdgcn_exp2f(ml[s].x - M);
      L = fmaf(w, ml[s].y, L); o0 = fmaf(w, a[s].x, o0); o1 = fmaf(w, a[s].y, o1);
    }
    const float inv = 1.f / L;
    *reinterpret_cast<unsigned*>(&o16[bb][g * 128 + d0]) = cvtpk(o0 * inv, o1 * inv);
  }
  __syncthreads();
  const u32x4 sel = make_sel(lane);
  f32x4 acc = {0.f, 0.f, 0.f, 0.f};
#pragma unroll
  for (int i = 0; i < 16; ++i) {
    const int row = wave * 64 + 4 * i + r;
    const unsigned P2 = (unsigned)o16[0][row] | ((unsigned)o16[1][row] << 16);
    acc = sel_mfma(P2, sel, wr[i], acc);
  }
  if (c < 9) *reinterpret_cast<f32x4*>(&yp[wave][r >> 1][8 * c + 4 * (r & 1)]) = acc;
  __syncthreads();
  float* y = reinterpret_cast<float*>(ws + WS_Y);
  if (tid < 144) {
    const int bb = tid / 72, n = tid % 72;
    const float v = (yp[0][bb][n] + yp[1][bb][n]) + (yp[2][bb][n] + yp[3][bb][n]);
    __hip_atomic_store((gu32*)(y + ((size_t)(ch * HKV + kvh) * 2 + bb) * 72 + n), __float_as_uint(v), __ATOMIC_RELAXED, __HIP_MEMORY_SCOPE_AGENT);
  }
  asm volatile("s_waitcnt vmcnt(0)" ::: "memory");
  __syncthreads();
  if (tid == 0) {
    const unsigned old = __hip_atomic_fetch_add((gu32*)(ws + WS_CNT) + ch * 32, 1u, __ATOMIC_RELAXED, __HIP_MEMORY_SCOPE_AGENT);
    lastflag = (old == (unsigned)(HKV - 1)) ? 1u : 0u;
  }
  __syncthreads();
  if (lastflag == 0u) return;
  __builtin_amdgcn_fence(__ATOMIC_ACQUIRE, "wavefront");
  if (tid < 144) {
    const int bb = tid / 72, n = tid % 72;
    float v[HKV];
#pragma unroll
    for (int h = 0; h < HKV; ++h)
      v[h] = __uint_as_float(__hip_atomic_load((gu32*)(y + ((size_t)(ch * HKV + h) * 2 + bb) * 72 + n), __ATOMIC_RELAXED, __HIP_MEMORY_SCOPE_AGENT));
    float s = 0.f;
#pragma unroll
    for (int h = 0; h < HKV; ++h) s += v[h];
    out[bb * DM + 72 * ch + n] = (unsigned short)(cvtpk(s, s) & 0xffffu);
  }
}

extern "C" void kernel_launch(void* const* d_in, const int* in_sizes, int n_in, void* d_out, int out_size, void* d_ws,
                              size_t ws_size, hipStream_t stream) {
  const unsigned short* x  = (const unsigned short*)d_in[0];
  const unsigned short* kc = (const unsigned short*)d_in[1];
  const unsigned short* vc = (const unsigned short*)d_in[2];
  const unsigned short* wq = (const unsigned short*)d_in[3];
  const unsigned short* wo = (const unsigned short*)d_in[4];
  unsigned char* ws = (unsigned char*)d_ws;
  (void)hipFuncSetAttribute((const void*)k_main, hipFuncAttributeMaxDynamicSharedMemorySize, LDS_MAIN);
  hipLaunchKernelGGL(k_main, dim3(256), dim3(512), LDS_MAIN, stream, x, kc, vc, wq, ws);
  hipLaunchKernelGGL(k_tail, dim3(128), dim3(256), 0, stream, wo, ws, (unsigned short*)d_out);
}
```

```cpp
#include <hip/hip_runtime.h>
#include <hip/hip_bf16.h>
#include <stdint.h>

using u32x4  = __attribute__((ext_vector_type(4))) unsigned;
using f32x4  = __attribute__((ext_vector_type(4))) float;
using bf16x2 = __attribute__((ext_vector_type(2))) __bf16;
using bf16x8 = __attribute__((ext_vector_type(8))) __bf16;
typedef __attribute__((address_space(1))) unsigned gu32;
typedef __attribute__((address_space(1))) unsigned long long gu64;

constexpr int NB = 2, DM = 576, HQ = 32, HKV = 16, SK = 32768, DH = 128;
constexpr int NSPLIT = 8;
constexpr int KEYS_WG = SK / NSPLIT;
constexpr int NWAVE = 8;
constexpr int PAGE = 32;
constexpr int NIT = KEYS_WG / (PAGE * NWAVE);
constexpr int QCOLS = HQ * DH;
constexpr float C2 = 0.08838834764831845f * 1.4426950408889634f;

constexpr size_t WS_PML = 4096;
constexpr size_t WS_PO  = WS_PML + (size_t)NB * HKV * 2 * NSPLIT * 8;
constexpr size_t WS_Y   = WS_PO + (size_t)NB * HKV * 2 * NSPLIT * DH * 4;
constexpr int NGRAN = 8 * HKV * 2 * 72;

__device__ __forceinline__ float dot2(unsigned k, unsigned q, float acc) {
  return __builtin_amdgcn_fdot2_f32_bf16(__builtin_bit_cast(bf16x2, k), __builtin_bit_cast(bf16x2, q), acc, false);
}
__device__ __forceinline__ unsigned cvtpk(float lo, float hi) {
  unsigned r; asm("v_cvt_pk_bf16_f32 %0, %1, %2" : "=v"(r) : "v"(lo), "v"(hi)); return r;
}
template <int CTRL> __device__ __forceinline__ float dpp(float x) {
  return __builtin_bit_cast(float, __builtin_amdgcn_mov_dpp(__builtin_bit_cast(int, x), CTRL, 0xf, 0xf, true));
}
template <int CTRL> __device__ __forceinline__ unsigned dppu(unsigned x) {
  return (unsigned)__builtin_amdgcn_mov_dpp((int)x, CTRL, 0xf, 0xf, true);
}
constexpr int XOR1 = 0xB1, XOR2 = 0x4E, XOR7 = 0x141, XOR8 = 0x128, ROR4 = 0x124, BCAST = 0x150;
__device__ __forceinline__ float xrow16_max(float x) {
  auto s = __builtin_amdgcn_permlane16_swap(__float_as_uint(x), __float_as_uint(x), false, false);
  x = fmaxf(__uint_as_float(s[0]), __uint_as_float(s[1]));
  auto t = __builtin_amdgcn_permlane32_swap(__float_as_uint(x), __float_as_uint(x), false, false);
  return fmaxf(__uint_as_float(t[0]), __uint_as_float(t[1]));
}
__device__ __forceinline__ f32x4 sel_mfma(unsigned P2, const u32x4& sel, const u32x4& bfrag, f32x4 acc) {
  u32x4 a;
  a[0] = __builtin_amdgcn_perm(P2, P2, sel[0]);
  a[1] = __builtin_amdgcn_perm(P2, P2, sel[1]);
  a[2] = __builtin_amdgcn_perm(P2, P2, sel[2]);
  a[3] = __builtin_amdgcn_perm(P2, P2, sel[3]);
  return __builtin_amdgcn_mfma_f32_16x16x32_bf16(__builtin_bit_cast(bf16x8, a), __builtin_bit_cast(bf16x8, bfrag), acc, 0, 0, 0);
}
__device__ __forceinline__ u32x4 make_sel(int lane) {
  const int m = lane & 15, g = m >> 3, j = m & 7;
  const unsigned bytes = g ? 0x0302u : 0x0100u;
  const unsigned v = (j & 1) ? ((bytes << 16) | 0x0c0cu) : (0x0c0c0000u | bytes);
  u32x4 s;
  s[0] = ((j >> 1) == 0) ? v : 0x0c0c0c0cu;
  s[1] = ((j >> 1) == 1) ? v : 0x0c0c0c0cu;
  s[2] = ((j >> 1) == 2) ? v : 0x0c0c0c0cu;
  s[3] = ((j >> 1) == 3) ? v : 0x0c0c0c0cu;
  return s;
}
__device__ __forceinline__ void lds_barrier() {
  asm volatile("s_waitcnt lgkmcnt(0)" ::: "memory");
  __builtin_amdgcn_s_barrier();
  asm volatile("" ::: "memory");
}
struct TrueT { static constexpr bool value = true; };
struct FalseT { static constexpr bool value = false; };
__device__ __forceinline__ u32x4 ldnt(const u32x4* p) { return __builtin_nontemporal_load(p); }

constexpr int L_XT = 0;
constexpr int L_QP = 2304;
constexpr int L_MR = L_QP + 4096;
constexpr int MR_REC = 2 * DH + 4;
constexpr int L_END = L_MR + NWAVE * MR_REC * 4;
constexpr int LDS_MAIN = 84 * 1024;
static_assert(L_END <= LDS_MAIN, "lds");

__global__ void __launch_bounds__(512) k_main(const unsigned short* __restrict__ x, const unsigned short* __restrict__ kc,
                                              const unsigned short* __restrict__ vc, const unsigned short* __restrict__ wq,
                                              unsigned char* __restrict__ ws) {
  extern __shared__ __attribute__((aligned(16))) unsigned char smem[];
  const int tid = threadIdx.x, lane = tid & 63, wave = __builtin_amdgcn_readfirstlane(tid >> 6);
  const int r = lane >> 4, c = lane & 15;
  const int bid = blockIdx.x, xcd = bid & 7, idx = bid >> 3;
  const int kvh = 2 * xcd + (idx & 1), b = (idx >> 1) & 1, split = idx >> 2;

  unsigned xv = 0;
  if (tid < DM / 2) xv = reinterpret_cast<const unsigned*>(x)[b * (DM / 2) + tid];
  const int hf = wave & 1, rg = wave >> 1;
  const size_t kvbase = ((size_t)(b * HKV + kvh) * SK + (size_t)split * KEYS_WG + (size_t)wave * 4 + r) * DH + 8 * c;
  const u32x4* kp = reinterpret_cast<const u32x4*>(kc + kvbase);
  const u32x4* vp = reinterpret_cast<const u32x4*>(vc + kvbase);
  constexpr int ISTR = NWAVE * 4 * DH / 8;
  constexpr int PSTR = NWAVE * PAGE * DH / 8;
  u32x4 kr[8], vr[8];
#pragma unroll
  for (int i = 0; i < 8; ++i) kr[i] = ldnt(kp + i * ISTR);
  __builtin_amdgcn_sched_barrier(0);
  const u32x4* wqp = reinterpret_cast<const u32x4*>(wq + (size_t)(rg * 144 + r) * QCOLS + kvh * 256 + hf * 128 + 8 * c);
  u32x4 wr[36];
#pragma unroll
  for (int i = 0; i < 36; ++i) wr[i] = wqp[(size_t)i * 4 * QCOLS / 8];
  __builtin_amdgcn_sched_barrier(0);
#pragma unroll
  for (int i = 0; i < 8; ++i) vr[i] = ldnt(vp + i * ISTR);
  __builtin_amdgcn_sched_barrier(0);

  if (tid < NGRAN / 256) reinterpret_cast<unsigned long long*>(ws + WS_Y)[bid * (NGRAN / 256) + tid] = 0ull;

  const u32x4 sel = make_sel(lane);
  unsigned* xt = reinterpret_cast<unsigned*>(smem + L_XT);
  if (tid < DM / 2) {
    const int k0 = 2 * tid, k1 = k0 + 1;
    xt[(k0 & 3) * 144 + (k0 >> 2)] = (xv & 0xffffu) | (xv << 16);
    xt[(k1 & 3) * 144 + (k1 >> 2)] = (xv >> 16) | (xv & 0xffff0000u);
  }
  lds_barrier();
  f32x4 qa = {0.f, 0.f, 0.f, 0.f};
  {
    const u32x4* xr = reinterpret_cast<const u32x4*>(xt + r * 144 + rg * 36);
#pragma unroll
    for (int u = 0; u < 9; ++u) {
      const u32x4 xx = xr[u];
#pragma unroll
      for (int e = 0; e < 4; ++e) qa = sel_mfma(xx[e], sel, wr[4 * u + e], qa);
    }
  }
  float* qp = reinterpret_cast<float*>(smem + L_QP);
  if (r < 2) *reinterpret_cast<f32x4*>(qp + wave * 128 + 8 * c + 4 * r) = qa;
  lds_barrier();
  u32x4 qpk[2];
#pragma unroll
  for (int g = 0; g < 2; ++g) {
    f32x4 lo = {0.f, 0.f, 0.f, 0.f}, hi = {0.f, 0.f, 0.f, 0.f};
#pragma unroll
    for (int w4 = 0; w4 < 4; ++w4) {
      lo += *reinterpret_cast<const f32x4*>(qp + (2 * w4 + g) * 128 + 8 * c);
      hi += *reinterpret_cast<const f32x4*>(qp + (2 * w4 + g) * 128 + 8 * c + 4);
    }
    qpk[g][0] = cvtpk(lo[0], lo[1]); qpk[g][1] = cvtpk(lo[2], lo[3]);
    qpk[g][2] = cvtpk(hi[0], hi[1]); qpk[g][3] = cvtpk(hi[2], hi[3]);
  }

  float m = -INFINITY, l = 0.f;
  f32x4 acc = {0.f, 0.f, 0.f, 0.f};
  auto step = [&](auto first_tag, auto more_tag) {
    constexpr bool FIRST = decltype(first_tag)::value, MORE = decltype(more_tag)::value;
    float xs[16];
#pragma unroll
    for (int i = 0; i < 8; ++i) {
#pragma unroll
      for (int g = 0; g < 2; ++g) {
        float d = 0.f;
#pragma unroll
        for (int k = 0; k < 4; ++k) d = dot2(kr[i][k], qpk[g][k], d);
        xs[2 * i + g] = d;
      }
      __builtin_amdgcn_sched_barrier(0);
      if constexpr (!FIRST) vr[i] = ldnt(vp + i * ISTR);
      __builtin_amdgcn_sched_barrier(0);
    }
#define TR_STEP(HALF, CTRL, BIT) _Pragma("unroll") for (int h = 0; h < (HALF); ++h) { \
      const float lo_ = xs[h], hi_ = xs[h + (HALF)]; const float t1_ = lo_ + dpp<CTRL>(lo_), t2_ = hi_ + dpp<CTRL>(hi_); \
      xs[h] = (c & (BIT)) ? t2_ : t1_; }
    TR_STEP(8, XOR8, 8) TR_STEP(4, XOR7, 4) TR_STEP(2, XOR2, 2) TR_STEP(1, XOR1, 1)
#undef TR_STEP
    const float sc = xs[0] * C2;
    float pm = fmaxf(sc, dpp<XOR2>(sc));
    pm = fmaxf(pm, dpp<ROR4>(pm));
    pm = fmaxf(pm, dpp<XOR8>(pm));
    pm = xrow16_max(pm);
    const float mn = fmaxf(m, pm);
    const float alpha = __builtin_amdgcn_exp2f(m - mn);
    const float p = __builtin_amdgcn_exp2f(sc - mn);
    l = fmaf(l, alpha, p);
    m = mn;
    const float a0 = dpp<0x00>(alpha), a1 = dpp<0x55>(alpha);
    const float aacc = (lane < 32) ? a0 : a1;
    acc *= aacc;
    const unsigned P2 = cvtpk(p, dpp<XOR1>(p));
    kp += PSTR;
#define PV_STEP(i) acc = sel_mfma(dppu<BCAST + 2 * (i)>(P2), sel, vr[i], acc); __builtin_amdgcn_sched_barrier(0); \
    if constexpr (MORE) { kr[i] = ldnt(kp + (i) * ISTR); } __builtin_amdgcn_sched_barrier(0);
    PV_STEP(0) PV_STEP(1) PV_STEP(2) PV_STEP(3) PV_STEP(4) PV_STEP(5) PV_STEP(6) PV_STEP(7)
#undef PV_STEP
    vp += PSTR;
  };
  step(TrueT{}, TrueT{});
#pragma unroll 1
  for (int it = 1; it < NIT - 1; ++it) step(FalseT{}, TrueT{});
  step(FalseT{}, FalseT{});
  l += dpp<XOR2>(l); l += dpp<ROR4>(l); l += dpp<XOR8>(l);
  {
    auto s = __builtin_amdgcn_permlane16_swap(__float_as_uint(l), __float_as_uint(l), false, false);
    l = __uint_as_float(s[0]) + __uint_as_float(s[1]);
    auto t = __builtin_amdgcn_permlane32_swap(__float_as_uint(l), __float_as_uint(l), false, false);
    l = __uint_as_float(t[0]) + __uint_as_float(t[1]);
  }
  float* mr = reinterpret_cast<float*>(smem + L_MR);
  {
    float* rec = mr + wave * MR_REC;
    *reinterpret_cast<f32x4*>(rec + 4 + (r >> 1) * 128 + 8 * c + 4 * (r & 1)) = acc;
    if (lane < 2) { rec[2 * lane] = m; rec[2 * lane + 1] = l; }
  }
  lds_barrier();
  if (tid < 256) {
    const int g = tid >> 7, d = tid & 127;
    float M = -INFINITY;
#pragma unroll
    for (int w = 0; w < NWAVE; ++w) M = fmaxf(M, mr[w * MR_REC + 2 * g]);
    float L = 0.f, o = 0.f;
#pragma unroll
    for (int w = 0; w < NWAVE; ++w) {
      const float wt = __builtin_amdgcn_exp2f(mr[w * MR_REC + 2 * g] - M);
      L = fmaf(wt, mr[w * MR_REC + 2 * g + 1], L);
      o = fmaf(wt, mr[w * MR_REC + 4 + g * 128 + d], o);
    }
    const size_t slot = (size_t)((b * HKV + kvh) * 2 + g) * NSPLIT + split;
    reinterpret_cast<float*>(ws + WS_PO)[slot * DH + d] = o;
    if (d == 0) reinterpret_cast<float2*>(ws + WS_PML)[slot] = make_float2(M, L);
  }
}

__global__ void __launch_bounds__(256) k_tail(const unsigned short* __restrict__ wo, unsigned char* __restrict__ ws,
                                              unsigned short* __restrict__ out) {
  __shared__ __attribute__((aligned(16))) unsigned short o16[2][256];
  __shared__ __attribute__((aligned(16))) float yp[4][2][128];
  const int tid = threadIdx.x, lane = tid & 63, wave = __builtin_amdgcn_readfirstlane(tid >> 6);
  const int r = lane >> 4, c = lane & 15;
  const int ch = blockIdx.x & 7, kvh = blockIdx.x >> 3;
  const int cc = c < 9 ? c : 8;
  const u32x4* wp = reinterpret_cast<const u32x4*>(wo + (size_t)(kvh * 256 + wave * 64 + r) * DM + 72 * ch + 8 * cc);
  u32x4 wr[16];
#pragma unroll
  for (int i = 0; i < 16; ++i) wr[i] = wp[(size_t)i * 4 * DM / 8];
  __builtin_amdgcn_sched_barrier(0);
  {
    const int bb = tid >> 7, g = (tid >> 6) & 1, d0 = 2 * (tid & 63);
    const size_t slot0 = (size_t)((bb * HKV + kvh) * 2 + g) * NSPLIT;
    const float2* pml = reinterpret_cast<const float2*>(ws + WS_PML) + slot0;
    const float* po = reinterpret_cast<const float*>(ws + WS_PO) + slot0 * DH + d0;
    float2 ml[NSPLIT], a[NSPLIT];
#pragma unroll
    for (int s = 0; s < NSPLIT; ++s) { ml[s] = pml[s]; a[s] = *reinterpret_cast<const float2*>(po + s * DH); }
    __builtin_amdgcn_sched_barrier(0);
    float M = -INFINITY;
#pragma unroll
    for (int s = 0; s < NSPLIT; ++s) M = fmaxf(M, ml[s].x);
    float L = 0.f, o0 = 0.f, o1 = 0.f;
#pragma unroll
    for (int s = 0; s < NSPLIT; ++s) {
      const float w = __builtin_amdgcn_exp2f(ml[s].x - M);
      L = fmaf(w, ml[s].y, L); o0 = fmaf(w, a[s].x, o0); o1 = fmaf(w, a[s].y, o1);
    }
    const float inv = 1.f / L;
    *reinterpret_cast<unsigned*>(&o16[bb][g * 128 + d0]) = cvtpk(o0 * inv, o1 * inv);
  }
  __syncthreads();
  const u32x4 sel = make_sel(lane);
  f32x4 acc = {0.f, 0.f, 0.f, 0.f};
#pragma unroll
  for (int i = 0; i < 16; ++i) {
    const int row = wave * 64 + 4 * i + r;
    const unsigned P2 = (unsigned)o16[0][row] | ((unsigned)o16[1][row] << 16);
    acc = sel_mfma(P2, sel, wr[i], acc);
  }
  if (c < 9) *reinterpret_cast<f32x4*>(&yp[wave][r >> 1][8 * c + 4 * (r & 1)]) = acc;
  __syncthreads();
  gu64* y = (gu64*)(ws + WS_Y);
  if (tid < 144) {
    const int bb = tid / 72, n = tid % 72;
    const float v = (yp[0][bb][n] + yp[1][bb][n]) + (yp[2][bb][n] + yp[3][bb][n]);
    if (kvh != HKV - 1) {
      __hip_atomic_store(y + ((size_t)(ch * HKV + kvh) * 144 + tid), (1ull << 32) | (unsigned long long)__float_as_uint(v),
                         __ATOMIC_RELAXED, __HIP_MEMORY_SCOPE_AGENT);
    } else {
      float g[HKV - 1];
      const gu64* src = y + (size_t)(ch * HKV) * 144 + tid;
      for (;;) {
        bool ok = true;
#pragma unroll
        for (int h = 0; h < HKV - 1; ++h) {
          const unsigned long long q = __hip_atomic_load(src + (size_t)h * 144, __ATOMIC_RELAXED, __HIP_MEMORY_SCOPE_AGENT);
          g[h] = __uint_as_float((unsigned)q);
          ok &= (q >> 32) == 1ull;
        }
        if (__all(ok)) break;
        __builtin_amdgcn_s_sleep(1);
      }
      float sum = 0.f;
#pragma unroll
      for (int h = 0; h < HKV - 1; ++h) sum += g[h];
      sum += v;
      out[bb * DM + 72 * ch + n] = (unsigned short)(cvtpk(sum, sum) & 0xffffu);
    }
  }
}

extern "C" void kernel_launch(void* const* d_in, const int* in_sizes, int n_in, void* d_out, int out_size, void* d_ws,
                              size_t ws_size, hipStream_t stream) {
  const unsigned short* x  = (const unsigned short*)d_in[0];
  const unsigned short* kc = (const unsigned short*)d_in[1];
  const unsigned short* vc = (const unsigned short*)d_in[2];
  const unsigned short* wq = (const unsigned short*)d_in[3];
  const unsigned short* wo = (const unsigned short*)d_in[4];
  unsigned char* ws = (unsigned char*)d_ws;
  (void)hipFuncSetAttribute((const void*)k_main, hipFuncAttributeMaxDynamicSharedMemorySize, LDS_MAIN);
  hipLaunchKernelGGL(k_main, dim3(256), dim3(512), LDS_MAIN, stream, x, kc, vc, wq, ws);
  hipLaunchKernelGGL(k_tail, dim3(128), dim3(256), 0, stream, wo, ws, (unsigned short*)d_out);
}
```

```cpp
#include <hip/hip_runtime.h>
#include <hip/hip_bf16.h>
#include <stdint.h>

using u32x4  = __attribute__((ext_vector_type(4))) unsigned;
using f32x4  = __attribute__((ext_vector_type(4))) float;
using bf16x2 = __attribute__((ext_vector_type(2))) __bf16;
using bf16x8 = __attribute__((ext_vector_type(8))) __bf16;
typedef __attribute__((address_space(1))) unsigned gu32;
typedef __attribute__((address_space(1))) unsigned long long gu64;

constexpr int NB = 2, DM = 576, HQ = 32, HKV = 16, SK = 32768, DH = 128;
constexpr int NSPLIT = 8;
constexpr int KEYS_WG = SK / NSPLIT;
constexpr int NWAVE = 8;
constexpr int PAGE = 32;
constexpr int NIT = KEYS_WG / (PAGE * NWAVE);
constexpr int QCOLS = HQ * DH;
constexpr float C2 = 0.08838834764831845f * 1.4426950408889634f;

constexpr size_t WS_CNT = 0;
constexpr size_t WS_PML = 4096;
constexpr size_t WS_PO  = WS_PML + (size_t)NB * HKV * 2 * NSPLIT * 8;
constexpr size_t WS_Y   = WS_PO + (size_t)NB * HKV * 2 * NSPLIT * DH * 4;

__device__ __forceinline__ float dot2(unsigned k, unsigned q, float acc) {
  return __builtin_amdgcn_fdot2_f32_bf16(__builtin_bit_cast(bf16x2, k), __builtin_bit_cast(bf16x2, q), acc, false);
}
__device__ __forceinline__ unsigned cvtpk(float lo, float hi) {
  unsigned r; asm("v_cvt_pk_bf16_f32 %0, %1, %2" : "=v"(r) : "v"(lo), "v"(hi)); return r;
}
template <int CTRL> __device__ __forceinline__ float dpp(float x) {
  return __builtin_bit_cast(float, __builtin_amdgcn_mov_dpp(__builtin_bit_cast(int, x), CTRL, 0xf, 0xf, true));
}
template <int CTRL> __device__ __forceinline__ unsigned dppu(unsigned x) {
  return (unsigned)__builtin_amdgcn_mov_dpp((int)x, CTRL, 0xf, 0xf, true);
}
constexpr int XOR1 = 0xB1, XOR2 = 0x4E, XOR7 = 0x141, XOR8 = 0x128, ROR4 = 0x124, BCAST = 0x150;
__device__ __forceinline__ float xrow16_max(float x) {
  auto s = __builtin_amdgcn_permlane16_swap(__float_as_uint(x), __float_as_uint(x), false, false);
  x = fmaxf(__uint_as_float(s[0]), __uint_as_float(s[1]));
  auto t = __builtin_amdgcn_permlane32_swap(__float_as_uint(x), __float_as_uint(x), false, false);
  return fmaxf(__uint_as_float(t[0]), __uint_as_float(t[1]));
}
__device__ __forceinline__ f32x4 sel_mfma(unsigned P2, const u32x4& sel, const u32x4& bfrag, f32x4 acc) {
  u32x4 a;
  a[0] = __builtin_amdgcn_perm(P2, P2, sel[0]);
  a[1] = __builtin_amdgcn_perm(P2, P2, sel[1]);
  a[2] = __builtin_amdgcn_perm(P2, P2, sel[2]);
  a[3] = __builtin_amdgcn_perm(P2, P2, sel[3]);
  return __builtin_amdgcn_mfma_f32_16x16x32_bf16(__builtin_bit_cast(bf16x8, a), __builtin_bit_cast(bf16x8, bfrag), acc, 0, 0, 0);
}
__device__ __forceinline__ u32x4 make_sel(int lane) {
  const int m = lane & 15, g = m >> 3, j = m & 7;
  const unsigned bytes = g ? 0x0302u : 0x0100u;
  const unsigned v = (j & 1) ? ((bytes << 16) | 0x0c0cu) : (0x0c0c0000u | bytes);
  u32x4 s;
  s[0] = ((j >> 1) == 0) ? v : 0x0c0c0c0cu;
  s[1] = ((j >> 1) == 1) ? v : 0x0c0c0c0cu;
  s[2] = ((j >> 1) == 2) ? v : 0x0c0c0c0cu;
  s[3] = ((j >> 1) == 3) ? v : 0x0c0c0c0cu;
  return s;
}
__device__ __forceinline__ void lds_barrier() {
  asm volatile("s_waitcnt lgkmcnt(0)" ::: "memory");
  __builtin_amdgcn_s_barrier();
  asm volatile("" ::: "memory");
}
struct TrueT { static constexpr bool value = true; };
struct FalseT { static constexpr bool value = false; };
__device__ __forceinline__ u32x4 ldnt(const u32x4* p) { return __builtin_nontemporal_load(p); }

constexpr int L_XT = 0;
constexpr int L_QP = 2304;
constexpr int L_MR = L_QP + 4096;
constexpr int MR_REC = 2 * DH + 4;
constexpr int L_END = L_MR + NWAVE * MR_REC * 4;
constexpr int LDS_MAIN = 84 * 1024;
static_assert(L_END <= LDS_MAIN, "lds");

__global__ void __launch_bounds__(512) k_main(const unsigned short* __restrict__ x, const unsigned short* __restrict__ kc,
                                              const unsigned short* __restrict__ vc, const unsigned short* __restrict__ wq,
                                              unsigned char* __restrict__ ws) {
  extern __shared__ __attribute__((aligned(16))) unsigned char smem[];
  const int tid = threadIdx.x, lane = tid & 63, wave = __builtin_amdgcn_readfirstlane(tid >> 6);
  const int r = lane >> 4, c = lane & 15;
  const int bid = blockIdx.x, xcd = bid & 7, idx = bid >> 3;
  const int kvh = 2 * xcd + (idx & 1), b = (idx >> 1) & 1, split = idx >> 2;

  unsigned xv = 0;
  if (tid < DM / 2) xv = reinterpret_cast<const unsigned*>(x)[b * (DM / 2) + tid];
  const int hf = wave & 1, rg = wave >> 1;
  const u32x4* wqp = reinterpret_cast<const u32x4*>(wq + (size_t)(rg * 144 + r) * QCOLS + kvh * 256 + hf * 128 + 8 * c);
  u32x4 wr[36];
#pragma unroll
  for (int i = 0; i < 36; ++i) wr[i] = wqp[(size_t)i * 4 * QCOLS / 8];
  __builtin_amdgcn_sched_barrier(0);
  const size_t kvbase = ((size_t)(b * HKV + kvh) * SK + (size_t)split * KEYS_WG + (size_t)wave * PAGE + r) * DH + 8 * c;
  const u32x4* kp = reinterpret_cast<const u32x4*>(kc + kvbase);
  const u32x4* vp = reinterpret_cast<const u32x4*>(vc + kvbase);
  constexpr int ISTR = 4 * DH / 8;
  constexpr int PSTR = NWAVE * PAGE * DH / 8;
  u32x4 kr[8], vr[8];
#pragma unroll
  for (int i = 0; i < 8; ++i) kr[i] = ldnt(kp + i * ISTR);
#pragma unroll
  for (int i = 0; i < 8; ++i) vr[i] = ldnt(vp + i * ISTR);
  __builtin_amdgcn_sched_barrier(0);

  if (bid == 0 && tid < 8) reinterpret_cast<unsigned*>(ws + WS_CNT)[tid * 32] = 0u;

  const u32x4 sel = make_sel(lane);
  unsigned* xt = reinterpret_cast<unsigned*>(smem + L_XT);
  if (tid < DM / 2) {
    const int k0 = 2 * tid, k1 = k0 + 1;
    xt[(k0 & 3) * 144 + (k0 >> 2)] = (xv & 0xffffu) | (xv << 16);
    xt[(k1 & 3) * 144 + (k1 >> 2)] = (xv >> 16) | (xv & 0xffff0000u);
  }
  lds_barrier();
  f32x4 qa = {0.f, 0.f, 0.f, 0.f};
  {
    const u32x4* xr = reinterpret_cast<const u32x4*>(xt + r * 144 + rg * 36);
#pragma unroll
    for (int u = 0; u < 9; ++u) {
      const u32x4 xx = xr[u];
#pragma unroll
      for (int e = 0; e < 4; ++e) qa = sel_mfma(xx[e], sel, wr[4 * u + e], qa);
    }
  }
  float* qp = reinterpret_cast<float*>(smem + L_QP);
  if (r < 2) *reinterpret_cast<f32x4*>(qp + wave * 128 + 8 * c + 4 * r) = qa;
  lds_barrier();
  u32x4 qpk[2];
#pragma unroll
  for (int g = 0; g < 2; ++g) {
    f32x4 lo = {0.f, 0.f, 0.f, 0.f}, hi = {0.f, 0.f, 0.f, 0.f};
#pragma unroll
    for (int w4 = 0; w4 < 4; ++w4) {
      lo += *reinterpret_cast<const f32x4*>(qp + (2 * w4 + g) * 128 + 8 * c);
      hi += *reinterpret_cast<const f32x4*>(qp + (2 * w4 + g) * 128 + 8 * c + 4);
    }
    qpk[g][0] = cvtpk(lo[0], lo[1]); qpk[g][1] = cvtpk(lo[2], lo[3]);
    qpk[g][2] = cvtpk(hi[0], hi[1]); qpk[g][3] = cvtpk(hi[2], hi[3]);
  }

  float m = -INFINITY, l = 0.f;
  f32x4 acc = {0.f, 0.f, 0.f, 0.f};
  auto step = [&](auto more_tag) {
    constexpr bool MORE = decltype(more_tag)::value;
    float xs[16];
#pragma unroll
    for (int i = 0; i < 8; ++i)
#pragma unroll
      for (int g = 0; g < 2; ++g) {
        float d = 0.f;
#pragma unroll
        for (int k = 0; k < 4; ++k) d = dot2(kr[i][k], qpk[g][k], d);
        xs[2 * i + g] = d;
      }
    __builtin_amdgcn_sched_barrier(0);
    if constexpr (MORE) {
      kp += PSTR;
#pragma unroll
      for (int i = 0; i < 8; ++i) kr[i] = ldnt(kp + i * ISTR);
    }
    __builtin_amdgcn_sched_barrier(0);
#define TR_STEP(HALF, CTRL, BIT) _Pragma("unroll") for (int h = 0; h < (HALF); ++h) { \
      const float lo_ = xs[h], hi_ = xs[h + (HALF)]; const float t1_ = lo_ + dpp<CTRL>(lo_), t2_ = hi_ + dpp<CTRL>(hi_); \
      xs[h] = (c & (BIT)) ? t2_ : t1_; }
    TR_STEP(8, XOR8, 8) TR_STEP(4, XOR7, 4) TR_STEP(2, XOR2, 2) TR_STEP(1, XOR1, 1)
#undef TR_STEP
    const float sc = xs[0] * C2;
    float pm = fmaxf(sc, dpp<XOR2>(sc));
    pm = fmaxf(pm, dpp<ROR4>(pm));
    pm = fmaxf(pm, dpp<XOR8>(pm));
    pm = xrow16_max(pm);
    const float mn = fmaxf(m, pm);
    const float alpha = __builtin_amdgcn_exp2f(m - mn);
    const float p = __builtin_amdgcn_exp2f(sc - mn);
    l = fmaf(l, alpha, p);
    m = mn;
    const float a0 = dpp<0x00>(alpha), a1 = dpp<0x55>(alpha);
    const float aacc = (lane < 32) ? a0 : a1;
    acc *= aacc;
    const unsigned P2 = cvtpk(p, dpp<XOR1>(p));
    acc = sel_mfma(dppu<BCAST + 0>(P2), sel, vr[0], acc);
    acc = sel_mfma(dppu<BCAST + 2>(P2), sel, vr[1], acc);
    acc = sel_mfma(dppu<BCAST + 4>(P2), sel, vr[2], acc);
    acc = sel_mfma(dppu<BCAST + 6>(P2), sel, vr[3], acc);
    acc = sel_mfma(dppu<BCAST + 8>(P2), sel, vr[4], acc);
    acc = sel_mfma(dppu<BCAST + 10>(P2), sel, vr[5], acc);
    acc = sel_mfma(dppu<BCAST + 12>(P2), sel, vr[6], acc);
    acc = sel_mfma(dppu<BCAST + 14>(P2), sel, vr[7], acc);
    __builtin_amdgcn_sched_barrier(0);
    if constexpr (MORE) {
      vp += PSTR;
#pragma unroll
      for (int i = 0; i < 8; ++i) vr[i] = ldnt(vp + i * ISTR);
    }
    __builtin_amdgcn_sched_barrier(0);
  };
#pragma unroll 1
  for (int it = 0; it < NIT - 1; ++it) step(TrueT{});
  step(FalseT{});
  l += dpp<XOR2>(l); l += dpp<ROR4>(l); l += dpp<XOR8>(l);
  {
    auto s = __builtin_amdgcn_permlane16_swap(__float_as_uint(l), __float_as_uint(l), false, false);
    l = __uint_as_float(s[0]) + __uint_as_float(s[1]);
    auto t = __builtin_amdgcn_permlane32_swap(__float_as_uint(l), __float_as_uint(l), false, false);
    l = __uint_as_float(t[0]) + __uint_as_float(t[1]);
  }
  float* mr = reinterpret_cast<float*>(smem + L_MR);
  {
    float* rec = mr + wave * MR_REC;
    *reinterpret_cast<f32x4*>(rec + 4 + (r >> 1) * 128 + 8 * c + 4 * (r & 1)) = acc;
    if (lane < 2) { rec[2 * lane] = m; rec[2 * lane + 1] = l; }
  }
  lds_barrier();
  if (tid < 256) {
    const int g = tid >> 7, d = tid & 127;
    float M = -INFINITY;
#pragma unroll
    for (int w = 0; w < NWAVE; ++w) M = fmaxf(M, mr[w * MR_REC + 2 * g]);
    float L = 0.f, o = 0.f;
#pragma unroll
    for (int w = 0; w < NWAVE; ++w) {
      const float wt = __builtin_amdgcn_exp2f(mr[w * MR_REC + 2 * g] - M);
      L = fmaf(wt, mr[w * MR_REC + 2 * g + 1], L);
      o = fmaf(wt, mr[w * MR_REC + 4 + g * 128 + d], o);
    }
    const size_t slot = (size_t)((b * HKV + kvh) * 2 + g) * NSPLIT + split;
    reinterpret_cast<float*>(ws + WS_PO)[slot * DH + d] = o;
    if (d == 0) reinterpret_cast<float2*>(ws + WS_PML)[slot] = make_float2(M, L);
  }
}

__global__ void __launch_bounds__(256) k_tail(const unsigned short* __restrict__ wo, unsigned char* __restrict__ ws,
                                              unsigned short* __restrict__ out) {
  __shared__ __attribute__((aligned(16))) unsigned short o16[2][256];
  __shared__ __attribute__((aligned(16))) float yp[4][2][128];
  __shared__ unsigned lastflag;
  const int tid = threadIdx.x, lane = tid & 63, wave = __builtin_amdgcn_readfirstlane(tid >> 6);
  const int r = lane >> 4, c = lane & 15;
  const int ch = blockIdx.x & 7, kvh = blockIdx.x >> 3;
  const int cc = c < 9 ? c : 8;
  const u32x4* wp = reinterpret_cast<const u32x4*>(wo + (size_t)(kvh * 256 + wave * 64 + r) * DM + 72 * ch + 8 * cc);
  u32x4 wr[16];
#pragma unroll
  for (int i = 0; i < 16; ++i) wr[i] = wp[(size_t)i * 4 * DM / 8];
  {
    const int bb = tid >> 7, g = (tid >> 6) & 1, d0 = 2 * (tid & 63);
    const size_t slot0 = (size_t)((bb * HKV + kvh) * 2 + g) * NSPLIT;
    const float2* pml = reinterpret_cast<const float2*>(ws + WS_PML) + slot0;
    const float* po = reinterpret_cast<const float*>(ws + WS_PO) + slot0 * DH + d0;
    float2 ml[NSPLIT], a[NSPLIT];
#pragma unroll
    for (int s = 0; s < NSPLIT; ++s) { ml[s] = pml[s]; a[s] = *reinterpret_cast<const float2*>(po + s * DH); }
    float M = -INFINITY;
#pragma unroll
    for (int s = 0; s < NSPLIT; ++s) M = fmaxf(M, ml[s].x);
    float L = 0.f, o0 = 0.f, o1 = 0.f;
#pragma unroll
    for (int s = 0; s < NSPLIT; ++s) {
      const float w = __builtin_amdgcn_exp2f(ml[s].x - M);
      L = fmaf(w, ml[s].y, L); o0 = fmaf(w, a[s].x, o0); o1 = fmaf(w, a[s].y, o1);
    }
    const float inv = 1.f / L;
    *reinterpret_cast<unsigned*>(&o16[bb][g * 128 + d0]) = cvtpk(o0 * inv, o1 * inv);
  }
  __syncthreads();
  const u32x4 sel = make_sel(lane);
  f32x4 acc = {0.f, 0.f, 0.f, 0.f};
#pragma unroll
  for (int i = 0; i < 16; ++i) {
    const int row = wave * 64 + 4 * i + r;
    const unsigned P2 = (unsigned)o16[0][row] | ((unsigned)o16[1][row] << 16);
    acc = sel_mfma(P2, sel, wr[i], acc);
  }
  if (c < 9) *reinterpret_cast<f32x4*>(&yp[wave][r >> 1][8 * c + 4 * (r & 1)]) = acc;
  __syncthreads();
  float* y = reinterpret_cast<float*>(ws + WS_Y);
  if (tid < 144) {
    const int bb = tid / 72, n = tid % 72;
    const float v = (yp[0][bb][n] + yp[1][bb][n]) + (yp[2][bb][n] + yp[3][bb][n]);
    __hip_atomic_store((gu32*)(y + ((size_t)(ch * HKV + kvh) * 2 + bb) * 72 + n), __float_as_uint(v), __ATOMIC_RELAXED, __HIP_MEMORY_SCOPE_AGENT);
  }
  asm volatile("s_waitcnt vmcnt(0)" ::: "memory");
  __syncthreads();
  if (tid == 0) {
    const unsigned old = __hip_atomic_fetch_add((gu32*)(ws + WS_CNT) + ch * 32, 1u, __ATOMIC_RELAXED, __HIP_MEMORY_SCOPE_AGENT);
    lastflag = (old == (unsigned)(HKV - 1)) ? 1u : 0u;
  }
  __syncthreads();
  if (lastflag == 0u) return;
  __builtin_amdgcn_fence(__ATOMIC_ACQUIRE, "wavefront");
  if (tid < 144) {
    const int bb = tid / 72, n = tid % 72;
    float v[HKV];
#pragma unroll
    for (int h = 0; h < HKV; ++h)
      v[h] = __uint_as_float(__hip_atomic_load((gu32*)(y + ((size_t)(ch * HKV + h) * 2 + bb) * 72 + n), __ATOMIC_RELAXED, __HIP_MEMORY_SCOPE_AGENT));
    float s = 0.f;
#pragma unroll
    for (int h = 0; h < HKV; ++h) s += v[h];
    out[bb * DM + 72 * ch + n] = (unsigned short)(cvtpk(s, s) & 0xffffu);
  }
}

extern "C" void kernel_launch(void* const* d_in, const int* in_sizes, int n_in, void* d_out, int out_size, void* d_ws,
                              size_t ws_size, hipStream_t stream) {
  const unsigned short* x  = (const unsigned short*)d_in[0];
  const unsigned short* kc = (const unsigned short*)d_in[1];
  const unsigned short* vc = (const unsigned short*)d_in[2];
  const unsigned short* wq = (const unsigned short*)d_in[3];
  const unsigned short* wo = (const unsigned short*)d_in[4];
  unsigned char* ws = (unsigned char*)d_ws;
  (void)hipFuncSetAttribute((const void*)k_main, hipFuncAttributeMaxDynamicSharedMemorySize, LDS_MAIN);
  hipLaunchKernelGGL(k_main, dim3(256), dim3(512), LDS_MAIN, stream, x, kc, vc, wq, ws);
  hipLaunchKernelGGL(k_tail, dim3(128), dim3(256), 0, stream, wo, ws, (unsigned short*)d_out);
}
```

```cpp
#define SLP 8
#include <hip/hip_runtime.h>
#include <hip/hip_bf16.h>
#include <stdint.h>

using u32x4  = __attribute__((ext_vector_type(4))) unsigned;
using f32x4  = __attribute__((ext_vector_type(4))) float;
using bf16x2 = __attribute__((ext_vector_type(2))) __bf16;
using bf16x8 = __attribute__((ext_vector_type(8))) __bf16;
typedef __attribute__((address_space(1))) unsigned gu32;
typedef __attribute__((address_space(1))) unsigned long long gu64;

constexpr int NB = 2, DM = 576, HQ = 32, HKV = 16, SK = 32768, DH = 128;
constexpr int NSPLIT = 8;
constexpr int KEYS_WG = SK / NSPLIT;
constexpr int NWAVE = 8;
constexpr int PAGE = 32;
constexpr int NIT = KEYS_WG / (PAGE * NWAVE);
constexpr int WIN = 4;
constexpr int QCOLS = HQ * DH;
constexpr float C2 = 0.08838834764831845f * 1.4426950408889634f;

constexpr int PG_REC = 132;
constexpr size_t WS_PG = 0;
constexpr size_t WS_Y  = WS_PG + (size_t)NB * HKV * 2 * NSPLIT * PG_REC * 8;
constexpr size_t WS_ZEND = WS_Y + (size_t)NB * NSPLIT * HKV * 72 * 8;
constexpr size_t WS_Q  = WS_ZEND;
static_assert(WS_ZEND % 16 == 0, "zero range");

__device__ __forceinline__ float dot2(unsigned k, unsigned q, float acc) {
  return __builtin_amdgcn_fdot2_f32_bf16(__builtin_bit_cast(bf16x2, k), __builtin_bit_cast(bf16x2, q), acc, false);
}
__device__ __forceinline__ unsigned cvtpk(float lo, float hi) {
  unsigned r; asm("v_cvt_pk_bf16_f32 %0, %1, %2" : "=v"(r) : "v"(lo), "v"(hi)); return r;
}
template <int CTRL> __device__ __forceinline__ float dpp(float x) {
  return __builtin_bit_cast(float, __builtin_amdgcn_mov_dpp(__builtin_bit_cast(int, x), CTRL, 0xf, 0xf, true));
}
template <int CTRL> __device__ __forceinline__ unsigned dppu(unsigned x) {
  return (unsigned)__builtin_amdgcn_mov_dpp((int)x, CTRL, 0xf, 0xf, true);
}
constexpr int XOR1 = 0xB1, XOR2 = 0x4E, XOR7 = 0x141, XOR8 = 0x128, ROR4 = 0x124, BCAST = 0x150;
__device__ __forceinline__ float xrow16_max(float x) {
  auto s = __builtin_amdgcn_permlane16_swap(__float_as_uint(x), __float_as_uint(x), false, false);
  x = fmaxf(__uint_as_float(s[0]), __uint_as_float(s[1]));
  auto t = __builtin_amdgcn_permlane32_swap(__float_as_uint(x), __float_as_uint(x), false, false);
  return fmaxf(__uint_as_float(t[0]), __uint_as_float(t[1]));
}
__device__ __forceinline__ f32x4 sel_mfma(unsigned P2, const u32x4& sel, const u32x4& bfrag, f32x4 acc) {
  u32x4 a;
  a[0] = __builtin_amdgcn_perm(P2, P2, sel[0]);
  a[1] = __builtin_amdgcn_perm(P2, P2, sel[1]);
  a[2] = __builtin_amdgcn_perm(P2, P2, sel[2]);
  a[3] = __builtin_amdgcn_perm(P2, P2, sel[3]);
  return __builtin_amdgcn_mfma_f32_16x16x32_bf16(__builtin_bit_cast(bf16x8, a), __builtin_bit_cast(bf16x8, bfrag), acc, 0, 0, 0);
}
__device__ __forceinline__ u32x4 make_sel(int lane) {
  const int m = lane & 15, g = m >> 3, j = m & 7;
  const unsigned bytes = g ? 0x0302u : 0x0100u;
  const unsigned v = (j & 1) ? ((bytes << 16) | 0x0c0cu) : (0x0c0c0000u | bytes);
  u32x4 s;
  s[0] = ((j >> 1) == 0) ? v : 0x0c0c0c0cu;
  s[1] = ((j >> 1) == 1) ? v : 0x0c0c0c0cu;
  s[2] = ((j >> 1) == 2) ? v : 0x0c0c0c0cu;
  s[3] = ((j >> 1) == 3) ? v : 0x0c0c0c0cu;
  return s;
}
__device__ __forceinline__ void lds_barrier() {
  asm volatile("s_waitcnt lgkmcnt(0)" ::: "memory");
  __builtin_amdgcn_s_barrier();
  asm volatile("" ::: "memory");
}
struct TrueT { static constexpr bool value = true; };
struct FalseT { static constexpr bool value = false; };
__device__ __forceinline__ u32x4 ldnt(const u32x4* p) { return __builtin_nontemporal_load(p); }

__global__ void __launch_bounds__(256) k_pre(const unsigned short* __restrict__ x, const unsigned short* __restrict__ wq,
                                             unsigned char* __restrict__ ws) {
  __shared__ __attribute__((aligned(16))) float yp[4][2][128];
  const int tid = threadIdx.x, lane = tid & 63, wave = __builtin_amdgcn_readfirstlane(tid >> 6);
  const int r = lane >> 4, c = lane & 15;
  const int cg = blockIdx.x & 31, rq = blockIdx.x >> 5;
  const int row0 = rq * 144 + wave * 36 + r;
  const u32x4* wp = reinterpret_cast<const u32x4*>(wq + (size_t)row0 * QCOLS + cg * 128 + 8 * c);
  u32x4 wr[9];
  unsigned P2[9];
#pragma unroll
  for (int i = 0; i < 9; ++i) wr[i] = wp[(size_t)i * 4 * QCOLS / 8];
#pragma unroll
  for (int i = 0; i < 9; ++i) P2[i] = (unsigned)x[row0 + 4 * i] | ((unsigned)x[DM + row0 + 4 * i] << 16);
  __builtin_amdgcn_sched_barrier(0);
  for (size_t o = ((size_t)blockIdx.x * 256 + tid) * 16; o < WS_ZEND; o += (size_t)128 * 256 * 16)
    *reinterpret_cast<u32x4*>(ws + o) = u32x4{0u, 0u, 0u, 0u};
  const u32x4 sel = make_sel(lane);
  f32x4 acc = {0.f, 0.f, 0.f, 0.f};
#pragma unroll
  for (int i = 0; i < 9; ++i) acc = sel_mfma(P2[i], sel, wr[i], acc);
  *reinterpret_cast<f32x4*>(&yp[wave][r >> 1][8 * c + 4 * (r & 1)]) = acc;
  __syncthreads();
  const int bb = tid >> 7, n = tid & 127;
  reinterpret_cast<float*>(ws + WS_Q)[(size_t)(rq * 2 + bb) * QCOLS + cg * 128 + n] =
      (yp[0][bb][n] + yp[1][bb][n]) + (yp[2][bb][n] + yp[3][bb][n]);
}

constexpr int L_MR = 0;
constexpr int MR_REC = 2 * DH + 4;
constexpr int L_O16 = L_MR + NWAVE * MR_REC * 4;
constexpr int L_YQ = L_O16 + 1024;
constexpr int L_END = L_YQ + 8 * 80 * 4;
constexpr int LDS_MAIN = 84 * 1024;
static_assert(L_END <= LDS_MAIN, "lds");

__global__ void __launch_bounds__(512) k_main(const unsigned short* __restrict__ kc, const unsigned short* __restrict__ vc,
                                              const unsigned short* __restrict__ wo, unsigned char* __restrict__ ws,
                                              unsigned short* __restrict__ out) {
  extern __shared__ __attribute__((aligned(16))) unsigned char smem[];
  const int tid = threadIdx.x, lane = tid & 63, wave = __builtin_amdgcn_readfirstlane(tid >> 6);
  const int r = lane >> 4, c = lane & 15;
  const int bid = blockIdx.x, xcd = bid & 7, idx = bid >> 3;
  const int kvh = 2 * xcd + (idx & 1), b = (idx >> 1) & 1, split = idx >> 2;
  f32x4 qf[4][2][2];
  {
    const float* qs = reinterpret_cast<const float*>(ws + WS_Q) + (size_t)b * QCOLS + kvh * 256 + 8 * c;
#pragma unroll
    for (int q4 = 0; q4 < 4; ++q4)
#pragma unroll
      for (int g = 0; g < 2; ++g) {
        qf[q4][g][0] = *reinterpret_cast<const f32x4*>(qs + (size_t)q4 * 2 * QCOLS + g * 128);
        qf[q4][g][1] = *reinterpret_cast<const f32x4*>(qs + (size_t)q4 * 2 * QCOLS + g * 128 + 4);
      }
  }
  __builtin_amdgcn_sched_barrier(0);
  const size_t kvbase = ((size_t)(b * HKV + kvh) * SK + (size_t)split * KEYS_WG + (size_t)wave * 4 + r) * DH + 8 * c;
  const u32x4* kp = reinterpret_cast<const u32x4*>(kc + kvbase);
  const u32x4* vp = reinterpret_cast<const u32x4*>(vc + kvbase);
  constexpr int ISTR = NWAVE * 4 * DH / 8;
  constexpr int PSTR = NWAVE * PAGE * DH / 8;
  u32x4 kr[8], vr[8];
#pragma unroll
  for (int i = 0; i < 8; ++i) kr[i] = ldnt(kp + i * ISTR);
#pragma unroll
  for (int i = 0; i < WIN; ++i) vr[i] = ldnt(vp + i * ISTR);
  __builtin_amdgcn_sched_barrier(0);
  const u32x4 sel = make_sel(lane);
  u32x4 qpk[2];
#pragma unroll
  for (int g = 0; g < 2; ++g) {
    const f32x4 lo = (qf[0][g][0] + qf[1][g][0]) + (qf[2][g][0] + qf[3][g][0]);
    const f32x4 hi = (qf[0][g][1] + qf[1][g][1]) + (qf[2][g][1] + qf[3][g][1]);
    qpk[g][0] = cvtpk(lo[0], lo[1]); qpk[g][1] = cvtpk(lo[2], lo[3]);
    qpk[g][2] = cvtpk(hi[0], hi[1]); qpk[g][3] = cvtpk(hi[2], hi[3]);
  }

  float m = -INFINITY, l = 0.f;
  f32x4 acc = {0.f, 0.f, 0.f, 0.f};
  unsigned P2 = 0;
#define PV_STEP(i, NEXT) acc = sel_mfma(dppu<BCAST + 2 * (i)>(P2), sel, vr[i], acc); __builtin_amdgcn_sched_barrier(0); \
    if constexpr ((i) + WIN < 8) { vr[((i) + WIN) & 7] = ldnt(vp + ((i) + WIN) * ISTR); } \
    else if constexpr (NEXT) { vr[((i) + WIN) & 7] = ldnt(vp + PSTR + ((i) + WIN - 8) * ISTR); } \
    __builtin_amdgcn_sched_barrier(0);
#define QK_STEP(i) { float d0 = 0.f, d1 = 0.f; \
    _Pragma("unroll") for (int k = 0; k < 4; ++k) { d0 = dot2(kr[i][k], qpk[0][k], d0); d1 = dot2(kr[i][k], qpk[1][k], d1); } \
    xs[2 * (i)] = d0; xs[2 * (i) + 1] = d1; __builtin_amdgcn_sched_barrier(0); \
    if constexpr ((i) + WIN < 8) { if constexpr (!FIRST) kr[((i) + WIN) & 7] = ldnt(kp + ((i) + WIN) * ISTR); } \
    else if constexpr (MORE) { kr[((i) + WIN) & 7] = ldnt(kp + PSTR + ((i) + WIN - 8) * ISTR); } \
    __builtin_amdgcn_sched_barrier(0); \
    if constexpr (!FIRST) { PV_STEP(i, true) } }
  auto step = [&](auto first_tag, auto more_tag) {
    constexpr bool FIRST = decltype(first_tag)::value, MORE = decltype(more_tag)::value;
    float xs[16];
    QK_STEP(0) QK_STEP(1) QK_STEP(2) QK_STEP(3) QK_STEP(4) QK_STEP(5) QK_STEP(6) QK_STEP(7)
    kp += PSTR;
    if constexpr (!FIRST) vp += PSTR;
#define TR_STEP(HALF, CTRL, BIT) _Pragma("unroll") for (int h = 0; h < (HALF); ++h) { \
      const float lo_ = xs[h], hi_ = xs[h + (HALF)]; const float t1_ = lo_ + dpp<CTRL>(lo_), t2_ = hi_ + dpp<CTRL>(hi_); \
      xs[h] = (c & (BIT)) ? t2_ : t1_; }
    TR_STEP(8, XOR8, 8) TR_STEP(4, XOR7, 4) TR_STEP(2, XOR2, 2) TR_STEP(1, XOR1, 1)
#undef TR_STEP
    const float sc = xs[0] * C2;
    float pm = fmaxf(sc, dpp<XOR2>(sc));
    pm = fmaxf(pm, dpp<ROR4>(pm));
    pm = fmaxf(pm, dpp<XOR8>(pm));
    pm = xrow16_max(pm);
    const float mn = fmaxf(m, pm);
    const float alpha = __builtin_amdgcn_exp2f(m - mn);
    const float p = __builtin_amdgcn_exp2f(sc - mn);
    l = fmaf(l, alpha, p);
    m = mn;
    const float a0 = dpp<0x00>(alpha), a1 = dpp<0x55>(alpha);
    const float aacc = (lane < 32) ? a0 : a1;
    acc *= aacc;
    P2 = cvtpk(p, dpp<XOR1>(p));
  };
  step(TrueT{}, TrueT{});
#pragma unroll 1
  for (int it = 1; it < NIT - 1; ++it) step(FalseT{}, TrueT{});
  u32x4 wr[8];
  {
    const u32x4* wp = reinterpret_cast<const u32x4*>(wo + (size_t)(kvh * 256 + wave * 32 + r) * DM + 72 * split + 8 * (c < 9 ? c : 8));
#pragma unroll
    for (int i = 0; i < 8; ++i) wr[i] = wp[(size_t)i * 4 * DM / 8];
  }
  __builtin_amdgcn_sched_barrier(0);
  step(FalseT{}, FalseT{});
  PV_STEP(0, false) PV_STEP(1, false) PV_STEP(2, false) PV_STEP(3, false)
  PV_STEP(4, false) PV_STEP(5, false) PV_STEP(6, false) PV_STEP(7, false)
#undef PV_STEP
#undef QK_STEP
  l += dpp<XOR2>(l); l += dpp<ROR4>(l); l += dpp<XOR8>(l);
  {
    auto s = __builtin_amdgcn_permlane16_swap(__float_as_uint(l), __float_as_uint(l), false, false);
    l = __uint_as_float(s[0]) + __uint_as_float(s[1]);
    auto t = __builtin_amdgcn_permlane32_swap(__float_as_uint(l), __float_as_uint(l), false, false);
    l = __uint_as_float(t[0]) + __uint_as_float(t[1]);
  }
  float* mr = reinterpret_cast<float*>(smem + L_MR);
  {
    float* rec = mr + wave * MR_REC;
    *reinterpret_cast<f32x4*>(rec + 4 + (r >> 1) * 128 + 8 * c + 4 * (r & 1)) = acc;
    if (lane < 2) { rec[2 * lane] = m; rec[2 * lane + 1] = l; }
  }
  lds_barrier();
  if (tid < 256) {
    const int g = tid >> 7, d = tid & 127;
    float M = -INFINITY;
#pragma unroll
    for (int w = 0; w < NWAVE; ++w) M = fmaxf(M, mr[w * MR_REC + 2 * g]);
    float L = 0.f, o = 0.f;
#pragma unroll
    for (int w = 0; w < NWAVE; ++w) {
      const float wt = __builtin_amdgcn_exp2f(mr[w * MR_REC + 2 * g] - M);
      L = fmaf(wt, mr[w * MR_REC + 2 * g + 1], L);
      o = fmaf(wt, mr[w * MR_REC + 4 + g * 128 + d], o);
    }
    gu64* pg = (gu64*)(ws + WS_PG) + (size_t)((b * HKV + kvh) * 2 + g) * NSPLIT * PG_REC;
    constexpr unsigned long long TAG = 1ull << 32;
    __hip_atomic_store(pg + split * PG_REC + d, TAG | __float_as_uint(o), __ATOMIC_RELAXED, __HIP_MEMORY_SCOPE_AGENT);
    if (d < 2) __hip_atomic_store(pg + split * PG_REC + 128 + d, TAG | __float_as_uint(d ? L : M), __ATOMIC_RELAXED, __HIP_MEMORY_SCOPE_AGENT);
    for (;;) {
      const unsigned long long q = __hip_atomic_load(pg + (lane & 7) * PG_REC + 128, __ATOMIC_RELAXED, __HIP_MEMORY_SCOPE_AGENT);
      if (__all((q >> 32) == 1ull)) break;
      __builtin_amdgcn_s_sleep(SLP);
    }
    float os[NSPLIT], ms[NSPLIT], ls[NSPLIT];
    for (;;) {
      bool ok = true;
#pragma unroll
      for (int s = 0; s < NSPLIT; ++s) {
        const unsigned long long qo = __hip_atomic_load(pg + s * PG_REC + d, __ATOMIC_RELAXED, __HIP_MEMORY_SCOPE_AGENT);
        const unsigned long long qm = __hip_atomic_load(pg + s * PG_REC + 128, __ATOMIC_RELAXED, __HIP_MEMORY_SCOPE_AGENT);
        const unsigned long long ql = __hip_atomic_load(pg + s * PG_REC + 129, __ATOMIC_RELAXED, __HIP_MEMORY_SCOPE_AGENT);
        os[s] = __uint_as_float((unsigned)qo); ms[s] = __uint_as_float((unsigned)qm); ls[s] = __uint_as_float((unsigned)ql);
        ok &= ((qo >> 32) == 1ull) & ((qm >> 32) == 1ull) & ((ql >> 32) == 1ull);
      }
      if (__all(ok)) break;
      __builtin_amdgcn_s_sleep(8);
    }
    float MM = -INFINITY;
#pragma unroll
    for (int s = 0; s < NSPLIT; ++s) MM = fmaxf(MM, ms[s]);
    float LL = 0.f, oo = 0.f;
#pragma unroll
    for (int s = 0; s < NSPLIT; ++s) {
      const float wt = __builtin_amdgcn_exp2f(ms[s] - MM);
      LL = fmaf(wt, ls[s], LL); oo = fmaf(wt, os[s], oo);
    }
    const float on = oo / LL;
    reinterpret_cast<unsigned*>(smem + L_O16)[tid] = cvtpk(on, on);
  }
  lds_barrier();
  {
    const unsigned* o16 = reinterpret_cast<const unsigned*>(smem + L_O16) + wave * 32 + r;
    f32x4 ya = {0.f, 0.f, 0.f, 0.f};
#pragma unroll
    for (int i = 0; i < 8; ++i) ya = sel_mfma(o16[4 * i], sel, wr[i], ya);
    if (r < 2 && c < 9) *reinterpret_cast<f32x4*>(reinterpret_cast<float*>(smem + L_YQ) + wave * 80 + 8 * c + 4 * r) = ya;
  }
  lds_barrier();
  if (tid < 72) {
    const float* yq = reinterpret_cast<const float*>(smem + L_YQ) + tid;
    const float v = ((yq[0] + yq[80]) + (yq[160] + yq[240])) + ((yq[320] + yq[400]) + (yq[480] + yq[560]));
    gu64* y = (gu64*)(ws + WS_Y) + (size_t)(b * NSPLIT + split) * HKV * 72 + tid;
    if (kvh != HKV - 1) {
      __hip_atomic_store(y + kvh * 72, (1ull << 32) | __float_as_uint(v), __ATOMIC_RELAXED, __HIP_MEMORY_SCOPE_AGENT);
    } else {
      float gv[HKV - 1];
      for (;;) {
        bool ok = true;
#pragma unroll
        for (int h = 0; h < HKV - 1; ++h) {
          const unsigned long long q = __hip_atomic_load(y + h * 72, __ATOMIC_RELAXED, __HIP_MEMORY_SCOPE_AGENT);
          gv[h] = __uint_as_float((unsigned)q);
          ok &= (q >> 32) == 1ull;
        }
        if (__all(ok)) break;
        __builtin_amdgcn_s_sleep(4);
      }
      float sum = 0.f;
#pragma unroll
      for (int h = 0; h < HKV - 1; ++h) sum += gv[h];
      sum += v;
      out[b * DM + 72 * split + tid] = (unsigned short)(cvtpk(sum, sum) & 0xffffu);
    }
  }
}

extern "C" void kernel_launch(void* const* d_in, const int* in_sizes, int n_in, void* d_out, int out_size, void* d_ws,
                              size_t ws_size, hipStream_t stream) {
  const unsigned short* x  = (const unsigned short*)d_in[0];
  const unsigned short* kc = (const unsigned short*)d_in[1];
  const unsigned short* vc = (const unsigned short*)d_in[2];
  const unsigned short* wq = (const unsigned short*)d_in[3];
  const unsigned short* wo = (const unsigned short*)d_in[4];
  unsigned char* ws = (unsigned char*)d_ws;
  (void)hipFuncSetAttribute((const void*)k_main, hipFuncAttributeMaxDynamicSharedMemorySize, LDS_MAIN);
  hipLaunchKernelGGL(k_pre, dim3(128), dim3(256), 0, stream, x, wq, ws);
  hipLaunchKernelGGL(k_main, dim3(256), dim3(512), LDS_MAIN, stream, kc, vc, wo, ws, (unsigned short*)d_out);
}
```
